```python
import torch

def _softmax(x, axis=-1):
    return torch.softmax(x, dim=axis)
def _gelu(x):
    return torch.nn.functional.gelu(x, approximate="tanh")
def _silu(x):
    return torch.nn.functional.silu(x)
def _sigmoid(x):
    return torch.sigmoid(x)
def _layernorm(x, axis=-1, eps=1e-5):
    m = x.mean(dim=axis, keepdim=True)
    v = x.var(dim=axis, keepdim=True, unbiased=False)
    return (x - m) / torch.sqrt(v + eps)
def _scatter_add(tgt, idx, src, axis=0):
    return tgt.clone().index_add_(axis, idx, src)
def _segment_sum(src, seg, n):
    out = torch.zeros((n,) + src.shape[1:], dtype=src.dtype)
    return out.index_add_(0, seg, src)
def _pad(x, pw):
    flat = tuple(v for pair in reversed(pw) for v in pair)
    return torch.nn.functional.pad(x, flat)

kernel_name = 'standalone__sliding_local_strided__prompt'

def setup_inputs():

    _w = (lambda a: (lambda t: t.to(torch.bfloat16) if t.is_floating_point() else t)(torch.as_tensor(a)))
    B, H, S, D = 1, 32, 2048, 128
    return {
        "q": _w((lambda *s: torch.randn(*s, dtype=torch.bfloat16) * 0.8)(B, H, S, D)),
        "k": _w((lambda *s: torch.randn(*s, dtype=torch.bfloat16) * 0.8)(B, H, S, D)),
        "v": _w((lambda *s: torch.randn(*s, dtype=torch.bfloat16) * 0.05)(B, H, S, D)),
    }

def reference(q, k, v):
    B, H, S, D = q.shape
    W, dil = 8, 4
    scale = D ** -0.5
    pos = torch.arange(S)
    kidx = pos[:, None] - torch.arange(W)[None, :] * dil
    valid = kidx >= 0
    kidx = torch.where(valid, kidx, torch.zeros_like(kidx))
    idx = torch.reshape(kidx, (1, 1, S * W, 1))
    idx = (lambda x, n, axis: torch.repeat_interleave(x, n, dim=axis))(idx, B, 0)
    idx = (lambda x, n, axis: torch.repeat_interleave(x, n, dim=axis))(idx, H, 1)
    idx = (lambda x, n, axis: torch.repeat_interleave(x, n, dim=axis))(idx, D, 3)
    kg = torch.reshape((lambda x, i, axis: torch.take_along_dim(x, i, dim=axis))(k, idx, 2), (B, H, S, W, D))
    vg = torch.reshape((lambda x, i, axis: torch.take_along_dim(x, i, dim=axis))(v, idx, 2), (B, H, S, W, D))
    scores = torch.einsum("bhsd,bhswd->bhsw", q, kg) * scale
    mask_f = (lambda x, dt: x.to(dt))(valid, q.dtype)[None, None, :, :]
    scores = scores + (mask_f - 1.0) * 1e9
    p = _softmax(scores, axis=-1)
    return torch.einsum("bhsw,bhswd->bhsd", p, vg)
```

```cpp
#include <hip/hip_runtime.h>
#include <hip/hip_bf16.h>
#include <stdint.h>

typedef __bf16 bf16x8 __attribute__((ext_vector_type(8)));
typedef __bf16 bf16x4 __attribute__((ext_vector_type(4)));
typedef __bf16 bf16x2 __attribute__((ext_vector_type(2)));
typedef float f32x4 __attribute__((ext_vector_type(4)));
typedef float f32x2 __attribute__((ext_vector_type(2)));
typedef unsigned u32x4 __attribute__((ext_vector_type(4)));
typedef unsigned u32x2 __attribute__((ext_vector_type(2)));

#define GAS __attribute__((address_space(1)))
#define LAS __attribute__((address_space(3)))

constexpr int H = 32, S = 2048, D = 128;
constexpr int ROWB = D * 2;
constexpr int CLS_STRIDE = 4 * ROWB;

#define TT 1

__device__ __forceinline__ float xrow_max(float x) {
    auto s = __builtin_amdgcn_permlane16_swap(__float_as_uint(x), __float_as_uint(x), false, false);
    x = fmaxf(__uint_as_float(s[0]), __uint_as_float(s[1]));
    auto t = __builtin_amdgcn_permlane32_swap(__float_as_uint(x), __float_as_uint(x), false, false);
    return fmaxf(__uint_as_float(t[0]), __uint_as_float(t[1]));
}
__device__ __forceinline__ float xrow_sum(float x) {
    auto s = __builtin_amdgcn_permlane16_swap(__float_as_uint(x), __float_as_uint(x), false, false);
    x = __uint_as_float(s[0]) + __uint_as_float(s[1]);
    auto t = __builtin_amdgcn_permlane32_swap(__float_as_uint(x), __float_as_uint(x), false, false);
    return __uint_as_float(t[0]) + __uint_as_float(t[1]);
}
__device__ __forceinline__ unsigned pk_bf16(float lo, float hi) {
    f32x2 v = {lo, hi};
    bf16x2 b = __builtin_convertvector(v, bf16x2);
    return __builtin_bit_cast(unsigned, b);
}

template <int T>
__global__ __launch_bounds__(256) void swa_dilated_kernel(const char* __restrict__ q, const char* __restrict__ k,
                                                          const char* __restrict__ v, char* __restrict__ o) {
    constexpr int VROWS = 16 * T + 8;
    constexpr int NWG = H * S / (64 * T);
    constexpr int BPH = S / (64 * T);
    __shared__ __attribute__((aligned(16))) char lds[4 * VROWS * ROWB];

    const int lane = threadIdx.x & 63;
    const int r = __builtin_amdgcn_readfirstlane(threadIdx.x >> 6);
    const int bid = blockIdx.x;
    const int wg = (bid & 7) * (NWG / 8) + (bid >> 3);
    const int h = wg / BPH, blk = wg % BPH;
    const int j0 = blk * 16 * T;
    const int n = lane & 15, g = lane >> 4;
    const size_t hb = (size_t)h * S * ROWB;

    char* vl = lds + r * (VROWS * ROWB);
    {
        const int chunk = n ^ (g << 2);
#pragma unroll
        for (int i = 0; i < VROWS / 4; ++i) {
            int pos = 4 * (j0 - 8 + 4 * i + g) + r;
            if (i < 2) pos = pos < 0 ? 0 : pos;
            const char* src = v + hb + (size_t)pos * ROWB + chunk * 16;
            __builtin_amdgcn_global_load_lds((const GAS void*)src, (LAS void*)(vl + i * 1024), 16, 0, 0);
        }
    }
    bf16x8 qf[T][4], kf[T + 1][4];
    {
        const int nn = n < 9 ? 9 : n;
        int pos = 4 * (j0 - 16 + nn) + r;
        pos = pos < 0 ? 0 : pos;
        const char* src = k + hb + (size_t)pos * ROWB + g * 16;
#pragma unroll
        for (int kk = 0; kk < 4; ++kk) kf[0][kk] = *(const bf16x8*)(src + kk * 64);
    }
#pragma unroll
    for (int t = 0; t < T; ++t) {
        const size_t off = hb + (size_t)(4 * (j0 + 16 * t + n) + r) * ROWB + g * 16;
#pragma unroll
        for (int kk = 0; kk < 4; ++kk) kf[t + 1][kk] = *(const bf16x8*)(k + off + kk * 64);
#pragma unroll
        for (int kk = 0; kk < 4; ++kk) qf[t][kk] = *(const bf16x8*)(q + off + kk * 64);
    }

    const float NEG = -__builtin_inff();
    f32x4 mprev, mcur;
#pragma unroll
    for (int i = 0; i < 4; ++i) {
        const int key = 4 * g + i;
        mcur[i] = ((unsigned)(n - key) <= 7u) ? 0.f : NEG;
        mprev[i] = (key >= n + 9) ? 0.f : NEG;
    }
    const float PRE = (blk == 0) ? -1e9f * 11.313708498984761f : 0.f;
    constexpr float C2 = 0.08838834764831845f * 1.4426950408889634f;

    const int tq = n >> 2, tp = n & 3;
    const unsigned vbase = (unsigned)(size_t)(LAS char*)vl;
    unsigned va[4];
#pragma unroll
    for (int t4 = 0; t4 < 4; ++t4) va[t4] = vbase + 256 * (4 * g + tq) + 64 * (t4 ^ tq) + 16 * tp;
    const unsigned vhalo = 256 * (4 * (g & 1) + tq) - 256 * (4 * g + tq);

    bf16x4 vprev[8];
#pragma unroll
    for (int t = 0; t < T; ++t) {
        f32x4 sp = mprev, sc = mcur;
        if (t == 0) {
#pragma unroll
            for (int i = 0; i < 4; ++i) sp[i] += PRE;
        }
#pragma unroll
        for (int kk = 0; kk < 4; ++kk) {
            sp = __builtin_amdgcn_mfma_f32_16x16x32_bf16(kf[t][kk], qf[t][kk], sp, 0, 0, 0);
            sc = __builtin_amdgcn_mfma_f32_16x16x32_bf16(kf[t + 1][kk], qf[t][kk], sc, 0, 0, 0);
        }
        float m = fmaxf(fmaxf(fmaxf(sp[0], sp[1]), fmaxf(sp[2], sp[3])), fmaxf(fmaxf(sc[0], sc[1]), fmaxf(sc[2], sc[3])));
        m = xrow_max(m);
        const float mb = -m * C2;
        float p[8];
#pragma unroll
        for (int i = 0; i < 4; ++i) {
            p[i] = __builtin_amdgcn_exp2f(fmaf(sp[i], C2, mb));
            p[4 + i] = __builtin_amdgcn_exp2f(fmaf(sc[i], C2, mb));
        }
        float l = ((p[0] + p[1]) + (p[2] + p[3])) + ((p[4] + p[5]) + (p[6] + p[7]));
        l = xrow_sum(l);
        const float inv = __builtin_amdgcn_rcpf(l);
        u32x4 pw = {pk_bf16(p[0] * inv, p[1] * inv), pk_bf16(p[2] * inv, p[3] * inv),
                    pk_bf16(p[4] * inv, p[5] * inv), pk_bf16(p[6] * inv, p[7] * inv)};
        const bf16x8 pf = __builtin_bit_cast(bf16x8, pw);

        char* orow = o + hb + (size_t)(4 * (j0 + 16 * t + n) + r) * ROWB + g * 16;
#pragma unroll
        for (int t4 = 0; t4 < 4; ++t4) {
            f32x4 acc[2];
#pragma unroll
            for (int u = 0; u < 2; ++u) {
                const int db = 2 * t4 + u;
                if (t == 0)
                    vprev[db] = __builtin_amdgcn_ds_read_tr16_b64_v4bf16((LAS bf16x4*)(size_t)(va[t4] + vhalo + 8 * u));
                const bf16x4 vc = __builtin_amdgcn_ds_read_tr16_b64_v4bf16(
                    (LAS bf16x4*)(size_t)(va[t4] + 8 * u + (8 + 16 * t) * 256));
                const bf16x8 a = __builtin_shufflevector(vprev[db], vc, 0, 1, 2, 3, 4, 5, 6, 7);
                vprev[db] = vc;
                acc[u] = __builtin_amdgcn_mfma_f32_16x16x32_bf16(a, pf, (f32x4){0.f, 0.f, 0.f, 0.f}, 0, 0, 0);
            }
            u32x4 w = {pk_bf16(acc[0][0], acc[0][1]), pk_bf16(acc[0][2], acc[0][3]),
                       pk_bf16(acc[1][0], acc[1][1]), pk_bf16(acc[1][2], acc[1][3])};
            *(u32x4*)(orow + 64 * t4) = w;
        }
    }
}

extern "C" void kernel_launch(void* const* d_in, const int* in_sizes, int n_in, void* d_out, int out_size,
                              void* d_ws, size_t ws_size, hipStream_t stream) {
    const char* q = (const char*)d_in[0];
    const char* k = (const char*)d_in[1];
    const char* v = (const char*)d_in[2];
    constexpr int T = TT;
    swa_dilated_kernel<T><<<H * S / (64 * T), 256, 0, stream>>>(q, k, v, (char*)d_out);
}
```

```cpp
#include <hip/hip_runtime.h>
#include <hip/hip_bf16.h>
#include <stdint.h>

typedef __bf16 bf16x8 __attribute__((ext_vector_type(8)));
typedef __bf16 bf16x4 __attribute__((ext_vector_type(4)));
typedef __bf16 bf16x2 __attribute__((ext_vector_type(2)));
typedef float f32x4 __attribute__((ext_vector_type(4)));
typedef float f32x2 __attribute__((ext_vector_type(2)));
typedef unsigned u32x4 __attribute__((ext_vector_type(4)));
typedef unsigned u32x8 __attribute__((ext_vector_type(8)));

#define LAS __attribute__((address_space(3)))
#define SBAR() __builtin_amdgcn_sched_barrier(0)

constexpr int H = 32, S = 2048, D = 128;
constexpr int ROWB = D * 2;
constexpr unsigned TBYTES = (unsigned)H * S * ROWB;
constexpr int T = 4;
constexpr int TILEB = 16 * 4 * ROWB;
constexpr int REGB = 4096;
constexpr int WAVE_LDS = 8 * REGB;
constexpr int LDS_BYTES = 4 * WAVE_LDS;
constexpr unsigned OOB = 0x80000000u;
constexpr int AUX_NT = 2;

__device__ __forceinline__ unsigned pk_bf16(float lo, float hi) {
    const f32x2 v = {lo, hi};
    return __builtin_bit_cast(unsigned, __builtin_convertvector(v, bf16x2));
}
__device__ __forceinline__ __amdgpu_buffer_rsrc_t mk_rsrc(unsigned lo, unsigned hi) {
    return __builtin_amdgcn_make_buffer_rsrc((void*)(((unsigned long long)hi << 32) | lo), 0, TBYTES, 0x00020000);
}

__device__ __forceinline__ void dma_tile(unsigned lds, __amdgpu_buffer_rsrc_t rs, unsigned soff, const unsigned (&v)[4]) {
    asm volatile("s_mov_b32 m0, %0\n\ts_nop 0\n\t"
                 "buffer_load_dwordx4 %1, %5, %6 offen nt lds\n\t"
                 "buffer_load_dwordx4 %2, %5, %6 offen offset:1024 nt lds\n\t"
                 "buffer_load_dwordx4 %3, %5, %6 offen offset:2048 nt lds\n\t"
                 "buffer_load_dwordx4 %4, %5, %6 offen offset:3072 nt lds"
                 :: "s"(lds), "v"(v[0]), "v"(v[1]), "v"(v[2]), "v"(v[3]), "s"(rs), "s"(soff) : "memory");
}
__device__ __forceinline__ void dma_edge(unsigned lds, __amdgpu_buffer_rsrc_t rs, unsigned soff, const unsigned (&v)[4]) {
    asm volatile("s_mov_b32 m0, %0\n\ts_nop 0\n\t"
                 "buffer_load_dwordx4 %1, %5, %6 offen nt lds\n\t"
                 "s_add_u32 m0, m0, 0x400\n\ts_nop 0\n\t"
                 "buffer_load_dwordx4 %2, %5, %6 offen nt lds\n\t"
                 "s_add_u32 m0, m0, 0x400\n\ts_nop 0\n\t"
                 "buffer_load_dwordx4 %3, %5, %6 offen nt lds\n\t"
                 "s_add_u32 m0, m0, 0x400\n\ts_nop 0\n\t"
                 "buffer_load_dwordx4 %4, %5, %6 offen nt lds"
                 :: "s"(lds), "v"(v[0]), "v"(v[1]), "v"(v[2]), "v"(v[3]), "s"(rs), "s"(soff) : "memory", "scc");
}
template <int N> __device__ __forceinline__ void wait_dma() { asm volatile("s_waitcnt vmcnt(%0)" :: "n"(N) : "memory"); }
__device__ __forceinline__ void wait_lds() { asm volatile("s_waitcnt lgkmcnt(0)" ::: "memory"); }

__device__ __forceinline__ bf16x4 read_vt(const unsigned (&va)[4], int db, int region) {
    return __builtin_amdgcn_ds_read_tr16_b64_v4bf16((LAS bf16x4*)(size_t)(va[db >> 1] + region * REGB + 8 * (db & 1)));
}
template <bool HI>
__device__ __forceinline__ void read_v(bf16x8 (&vt)[8], const unsigned (&va)[4], int region) {
#pragma unroll
    for (int db = 0; db < 8; ++db) {
        const bf16x4 vn = read_vt(va, db, region);
        const bf16x8 w = __builtin_shufflevector(vn, vn, 0, 1, 2, 3, 0, 1, 2, 3);
        vt[db] = HI ? __builtin_shufflevector(vt[db], w, 0, 1, 2, 3, 12, 13, 14, 15)
                    : __builtin_shufflevector(w, vt[db], 0, 1, 2, 3, 12, 13, 14, 15);
    }
}

__device__ __forceinline__ float row_max(const f32x4& a, const f32x4& b) {
    float m, t;
    asm volatile("s_nop 7\n\t"
                 "v_max3_f32 %0, %2, %3, %4\n\t"
                 "v_max3_f32 %1, %6, %7, %8\n\t"
                 "v_max3_f32 %0, %0, %5, %9\n\t"
                 "v_max_f32 %0, %0, %1"
                 : "=&v"(m), "=&v"(t)
                 : "v"(a[0]), "v"(a[1]), "v"(a[2]), "v"(a[3]), "v"(b[0]), "v"(b[1]), "v"(b[2]), "v"(b[3]));
    asm volatile("v_mov_b32 %1, %0\n\ts_nop 1\n\tv_permlane16_swap_b32 %0, %1\n\tv_max_f32 %0, %0, %1\n\t"
                 "v_mov_b32 %1, %0\n\ts_nop 1\n\tv_permlane32_swap_b32 %0, %1\n\tv_max_f32 %0, %0, %1"
                 : "+v"(m), "=&v"(t));
    return m;
}
__device__ __forceinline__ float row_sum(float x) {
    const auto s = __builtin_amdgcn_permlane16_swap(__float_as_uint(x), __float_as_uint(x), false, false);
    x = __uint_as_float(s[0]) + __uint_as_float(s[1]);
    const auto t = __builtin_amdgcn_permlane32_swap(__float_as_uint(x), __float_as_uint(x), false, false);
    return __uint_as_float(t[0]) + __uint_as_float(t[1]);
}

template <bool NEW_HI>
__device__ __forceinline__ bf16x8 scores_softmax(const bf16x8 (&kold)[4], const bf16x8 (&knew)[4], const bf16x8 (&qf)[4],
                                                 f32x4 so, f32x4 sn) {
    constexpr float C2 = 0.08838834764831845f * 1.4426950408889634f;
#pragma unroll
    for (int kk = 0; kk < 4; ++kk) {
        so = __builtin_amdgcn_mfma_f32_16x16x32_bf16(kold[kk], qf[kk], so, 0, 0, 0);
        sn = __builtin_amdgcn_mfma_f32_16x16x32_bf16(knew[kk], qf[kk], sn, 0, 0, 0);
    }
    const float mb = -row_max(so, sn) * C2;
    float p[8];
#pragma unroll
    for (int e = 0; e < 4; ++e) {
        p[e] = __builtin_amdgcn_exp2f(fmaf(so[e], C2, mb));
        p[4 + e] = __builtin_amdgcn_exp2f(fmaf(sn[e], C2, mb));
    }
    const float inv = __builtin_amdgcn_rcpf(row_sum(((p[0] + p[1]) + (p[2] + p[3])) + ((p[4] + p[5]) + (p[6] + p[7]))));
    const unsigned po0 = pk_bf16(p[0] * inv, p[1] * inv), po1 = pk_bf16(p[2] * inv, p[3] * inv);
    const unsigned pn0 = pk_bf16(p[4] * inv, p[5] * inv), pn1 = pk_bf16(p[6] * inv, p[7] * inv);
    const u32x4 pw = NEW_HI ? u32x4{po0, po1, pn0, pn1} : u32x4{pn0, pn1, po0, po1};
    bf16x8 pf = __builtin_bit_cast(bf16x8, pw);
    asm volatile("" : "+v"(pf));
    return pf;
}

__device__ __forceinline__ void pv_tile(u32x4 (&out)[4], const bf16x8 (&vt)[8], bf16x8 pf, const unsigned (&ow)[4], unsigned orr) {
#pragma unroll
    for (int t = 0; t < 4; ++t) {
        const f32x4 a0 = __builtin_amdgcn_mfma_f32_16x16x32_bf16(vt[2 * t], pf, (f32x4){0.f, 0.f, 0.f, 0.f}, 0, 0, 0);
        const f32x4 a1 = __builtin_amdgcn_mfma_f32_16x16x32_bf16(vt[2 * t + 1], pf, (f32x4){0.f, 0.f, 0.f, 0.f}, 0, 0, 0);
        const u32x4 w = {pk_bf16(a0[0], a0[1]), pk_bf16(a0[2], a0[3]), pk_bf16(a1[0], a1[1]), pk_bf16(a1[2], a1[3])};
        *(LAS u32x4*)(size_t)(ow[t]) = w;
    }
#pragma unroll
    for (int c = 0; c < 4; ++c) out[c] = *(const LAS u32x4*)(size_t)(orr + 1024 * c);
}
__device__ __forceinline__ void hold(u32x4 (&out)[4]) {
#pragma unroll
    for (int c = 0; c < 4; ++c) asm volatile("" : "+v"(out[c]));
}
__device__ __forceinline__ void store_tile(const u32x4 (&out)[4], __amdgpu_buffer_rsrc_t ro, const unsigned (&ost)[4], int off) {
#pragma unroll
    for (int c = 0; c < 4; ++c) __builtin_amdgcn_raw_buffer_store_b128(out[c], ro, ost[c] + (unsigned)off, 0, AUX_NT);
}

__global__ __launch_bounds__(256, 2) void swa_dilated(const char* __restrict__ q, const char* __restrict__ k,
                                                      const char* __restrict__ v, char* __restrict__ o) {
    extern __shared__ __attribute__((aligned(16))) char lds[];
    const u32x8 ka = *(const __attribute__((address_space(4))) u32x8*)__builtin_amdgcn_kernarg_segment_ptr();
    SBAR();
    const int lane = threadIdx.x & 63;
    const unsigned r = (unsigned)__builtin_amdgcn_readfirstlane(threadIdx.x >> 6);
    const unsigned bid = blockIdx.x;
    const unsigned wg = (bid & 7) * 32 + (bid >> 3);
    const unsigned blk = wg & 7;
    const bool asc = (blk & 1) != 0;
    const int n = lane & 15, g = lane >> 4;
    const unsigned hb = (wg >> 3) * (S * ROWB);
    const unsigned base = wg * (64 * T * ROWB) + r * ROWB;
    const int step = asc ? TILEB : -TILEB;
    const unsigned q0 = asc ? base : base + (T - 1) * TILEB;
    const unsigned kv1 = asc ? base : base + (T - 2) * TILEB;
    const unsigned wl = (unsigned)(size_t)(LAS char*)lds + r * WAVE_LDS;

    unsigned nk[4], nv[4], vqk[4], vv[4], hk[4], hv[4], fk[4], fv[4];
    const unsigned cv = (unsigned)((n ^ (g << 2)) << 4);
    const unsigned rowsel = blk == 0 ? 0u : 1u;
#pragma unroll
    for (int i = 0; i < 4; ++i) {
        const unsigned ck = (unsigned)(((n ^ g) ^ (4 * i)) << 4);
        const unsigned row = (unsigned)(g * 1024 + i * 4096);
        nk[i] = row + ck;
        nv[i] = row + cv;
        vqk[i] = nk[i] - 1024 * i;
        vv[i] = nv[i] - 1024 * i;
        hk[i] = i < 2 ? OOB : row * rowsel + ck;
        hv[i] = i < 2 ? OOB : row * rowsel + cv;
        fk[i] = asc ? hk[i] : nk[i];
        fv[i] = asc ? hv[i] : nv[i];
    }
    const unsigned halo = blk == 0 ? hb : base - TILEB;
    const unsigned first = asc ? halo : base + (T - 1) * TILEB;
    const unsigned last = asc ? base + (T - 1) * TILEB : halo;
    u32x8 kb = ka;
    asm volatile("" : "+s"(kb), "+v"(fk[0]), "+v"(fk[1]), "+v"(fk[2]), "+v"(fk[3]));
    const __amdgpu_buffer_rsrc_t rq = mk_rsrc(kb[0], kb[1]), rk = mk_rsrc(kb[2], kb[3]);
    const __amdgpu_buffer_rsrc_t rv = mk_rsrc(kb[4], kb[5]), ro = mk_rsrc(kb[6], kb[7]);

    dma_edge(wl + 0 * REGB, rk, first, fk);
    dma_edge(wl + 1 * REGB, rv, first, fv);
    dma_tile(wl + 2 * REGB, rk, kv1, vqk);
    dma_tile(wl + 3 * REGB, rq, q0, vqk);
    dma_tile(wl + 4 * REGB, rv, kv1, vv);
    dma_tile(wl + 5 * REGB, rk, kv1 + step, vqk);
    dma_tile(wl + 6 * REGB, rq, q0 + step, vqk);
    dma_tile(wl + 7 * REGB, rv, kv1 + step, vv);

    const float NEG = -__builtin_inff();
    f32x4 mold, mnew;
#pragma unroll
    for (int e = 0; e < 4; ++e) {
        const int key = 4 * g + e;
        const float mc = ((unsigned)(n - key) <= 7u) ? 0.f : NEG;
        const float mp = (key >= n + 9) ? 0.f : NEG;
        mold[e] = asc ? mp : mc;
        mnew[e] = asc ? mc : mp;
    }
    const float pre = (blk == 0) ? -1e9f * 11.313708498984761f : 0.f;
    const float pre_first = asc ? pre : 0.f, pre_last = asc ? 0.f : pre;

    unsigned fa[4], va[4], ow[4], ost[4];
    const int tq = n >> 2, tp = n & 3;
#pragma unroll
    for (int c = 0; c < 4; ++c) {
        fa[c] = wl + (unsigned)(256 * n + (((g ^ n) ^ (4 * c)) << 4));
        va[c] = wl + (unsigned)(256 * (4 * g + tq) + 64 * (c ^ tq) + 16 * tp);
        ow[c] = wl + 3 * REGB + (unsigned)(256 * n + (((4 * c + g) ^ (n & 7)) << 4));
        const int row = 4 * c + g;
        ost[c] = q0 + (unsigned)(row * 1024 + ((n ^ (row & 7)) << 4));
    }
    const unsigned orr = wl + 3 * REGB + (unsigned)(lane * 16);

    bf16x8 ka_[4], kb_[4], qf[4], vt[8], pf;
    u32x4 out0[4], out1[4], out2[4], out3[4];

    wait_dma<16>();
#pragma unroll
    for (int kk = 0; kk < 4; ++kk) {
        qf[kk] = *(const LAS bf16x8*)(size_t)(fa[kk] + 3 * REGB);
        ka_[kk] = *(const LAS bf16x8*)(size_t)(fa[kk] + 0 * REGB);
        kb_[kk] = *(const LAS bf16x8*)(size_t)(fa[kk] + 2 * REGB);
    }
    SBAR();
    wait_lds();
    dma_tile(wl + 0 * REGB, rv, kv1 + 2 * step, vv);
    pf = scores_softmax<true>(ka_, kb_, qf, mold + pre_first, mnew);
    wait_dma<16>();
#pragma unroll
    for (int db = 0; db < 8; ++db) {
        const bf16x4 vn = read_vt(va, db, 4), vo = read_vt(va, db, 1);
        vt[db] = __builtin_shufflevector(vo, vn, 0, 1, 2, 3, 4, 5, 6, 7);
    }
    SBAR();
    wait_lds();
    dma_tile(wl + 1 * REGB, rk, kv1 + 2 * step, vqk);
    dma_tile(wl + 2 * REGB, rq, q0 + 2 * step, vqk);
    pv_tile(out0, vt, pf, ow, orr);
    hold(out0);

    wait_dma<16>();
#pragma unroll
    for (int kk = 0; kk < 4; ++kk) {
        qf[kk] = *(const LAS bf16x8*)(size_t)(fa[kk] + 6 * REGB);
        ka_[kk] = *(const LAS bf16x8*)(size_t)(fa[kk] + 5 * REGB);
    }
    SBAR();
    wait_lds();
    {
        unsigned x[4];
#pragma unroll
        for (int i = 0; i < 4; ++i) x[i] = asc ? nv[i] : hv[i];
        dma_edge(wl + 4 * REGB, rv, last, x);
    }
    pf = scores_softmax<false>(kb_, ka_, qf, mold, mnew);
    wait_dma<16>();
    read_v<false>(vt, va, 7);
    SBAR();
    wait_lds();
    {
        unsigned x[4];
#pragma unroll
        for (int i = 0; i < 4; ++i) x[i] = asc ? nk[i] : hk[i];
        dma_edge(wl + 5 * REGB, rk, last, x);
    }
    dma_tile(wl + 6 * REGB, rq, q0 + 3 * step, vqk);
    pv_tile(out1, vt, pf, ow, orr);
    hold(out1);

    wait_dma<12>();
#pragma unroll
    for (int kk = 0; kk < 4; ++kk) {
        qf[kk] = *(const LAS bf16x8*)(size_t)(fa[kk] + 2 * REGB);
        kb_[kk] = *(const LAS bf16x8*)(size_t)(fa[kk] + 1 * REGB);
    }
    SBAR();
    pf = scores_softmax<true>(ka_, kb_, qf, mold, mnew);
    wait_dma<20>();
    read_v<true>(vt, va, 0);
    SBAR();
    pv_tile(out2, vt, pf, ow, orr);
    hold(out2);

    wait_dma<0>();
#pragma unroll
    for (int kk = 0; kk < 4; ++kk) {
        qf[kk] = *(const LAS bf16x8*)(size_t)(fa[kk] + 6 * REGB);
        ka_[kk] = *(const LAS bf16x8*)(size_t)(fa[kk] + 5 * REGB);
    }
    SBAR();
    pf = scores_softmax<false>(kb_, ka_, qf, mold, mnew + pre_last);
    read_v<false>(vt, va, 4);
    SBAR();
    pv_tile(out3, vt, pf, ow, orr);

    store_tile(out3, ro, ost, 3 * step);
    store_tile(out0, ro, ost, 0);
    store_tile(out1, ro, ost, step);
    store_tile(out2, ro, ost, 2 * step);
}

extern "C" void kernel_launch(void* const* d_in, const int* in_sizes, int n_in, void* d_out, int out_size,
                              void* d_ws, size_t ws_size, hipStream_t stream) {
    (void)hipFuncSetAttribute((const void*)swa_dilated, hipFuncAttributeMaxDynamicSharedMemorySize, LDS_BYTES);
    swa_dilated<<<H * S / (64 * T), 256, LDS_BYTES, stream>>>((const char*)d_in[0], (const char*)d_in[1],
                                                              (const char*)d_in[2], (char*)d_out);
}
```

```cpp
#include <hip/hip_runtime.h>
#include <hip/hip_bf16.h>
#include <stdint.h>

typedef __bf16 bf16x8 __attribute__((ext_vector_type(8)));
typedef __bf16 bf16x4 __attribute__((ext_vector_type(4)));
typedef __bf16 bf16x2 __attribute__((ext_vector_type(2)));
typedef float f32x4 __attribute__((ext_vector_type(4)));
typedef float f32x2 __attribute__((ext_vector_type(2)));
typedef unsigned u32x4 __attribute__((ext_vector_type(4)));
typedef unsigned u32x8 __attribute__((ext_vector_type(8)));

#define LAS __attribute__((address_space(3)))
#define SBAR() __builtin_amdgcn_sched_barrier(0)

constexpr int H = 32, S = 2048, D = 128;
constexpr int ROWB = D * 2;
constexpr unsigned TBYTES = (unsigned)H * S * ROWB;
constexpr int T = 4;
constexpr int TILEB = 16 * 4 * ROWB;
constexpr int REGB = 4096;
constexpr int WAVE_LDS = 8 * REGB;
constexpr int LDS_BYTES = 4 * WAVE_LDS;
constexpr unsigned OOB = 0x80000000u;
constexpr int AUX_NT = 2;

__device__ __forceinline__ unsigned pk_bf16(float lo, float hi) {
    const f32x2 v = {lo, hi};
    return __builtin_bit_cast(unsigned, __builtin_convertvector(v, bf16x2));
}
__device__ __forceinline__ __amdgpu_buffer_rsrc_t mk_rsrc(unsigned lo, unsigned hi) {
    return __builtin_amdgcn_make_buffer_rsrc((void*)(((unsigned long long)hi << 32) | lo), 0, TBYTES, 0x00020000);
}

__device__ __forceinline__ void dma_tile(unsigned lds, __amdgpu_buffer_rsrc_t rs, unsigned soff, const unsigned (&v)[4]) {
    asm volatile("s_mov_b32 m0, %0\n\ts_nop 0\n\t"
                 "buffer_load_dwordx4 %1, %5, %6 offen nt lds\n\t"
                 "buffer_load_dwordx4 %2, %5, %6 offen offset:1024 nt lds\n\t"
                 "buffer_load_dwordx4 %3, %5, %6 offen offset:2048 nt lds\n\t"
                 "buffer_load_dwordx4 %4, %5, %6 offen offset:3072 nt lds"
                 :: "s"(lds), "v"(v[0]), "v"(v[1]), "v"(v[2]), "v"(v[3]), "s"(rs), "s"(soff) : "memory");
}
__device__ __forceinline__ void dma_edge(unsigned lds, __amdgpu_buffer_rsrc_t rs, unsigned soff, const unsigned (&v)[4]) {
    asm volatile("s_mov_b32 m0, %0\n\ts_nop 0\n\t"
                 "buffer_load_dwordx4 %1, %5, %6 offen nt lds\n\t"
                 "s_add_u32 m0, m0, 0x400\n\ts_nop 0\n\t"
                 "buffer_load_dwordx4 %2, %5, %6 offen nt lds\n\t"
                 "s_add_u32 m0, m0, 0x400\n\ts_nop 0\n\t"
                 "buffer_load_dwordx4 %3, %5, %6 offen nt lds\n\t"
                 "s_add_u32 m0, m0, 0x400\n\ts_nop 0\n\t"
                 "buffer_load_dwordx4 %4, %5, %6 offen nt lds"
                 :: "s"(lds), "v"(v[0]), "v"(v[1]), "v"(v[2]), "v"(v[3]), "s"(rs), "s"(soff) : "memory", "scc");
}
template <int N> __device__ __forceinline__ void wait_dma() { asm volatile("s_waitcnt vmcnt(%0)" :: "n"(N) : "memory"); }
__device__ __forceinline__ void wait_lds() { asm volatile("s_waitcnt lgkmcnt(0)" ::: "memory"); }

__device__ __forceinline__ bf16x4 read_vt(const unsigned (&va)[4], int db, int region) {
    return __builtin_amdgcn_ds_read_tr16_b64_v4bf16((LAS bf16x4*)(size_t)(va[db >> 1] + region * REGB + 8 * (db & 1)));
}
template <bool HI>
__device__ __forceinline__ void read_v(bf16x8 (&vt)[8], const unsigned (&va)[4], int region) {
#pragma unroll
    for (int db = 0; db < 8; ++db) {
        const bf16x4 vn = read_vt(va, db, region);
        const bf16x8 w = __builtin_shufflevector(vn, vn, 0, 1, 2, 3, 0, 1, 2, 3);
        vt[db] = HI ? __builtin_shufflevector(vt[db], w, 0, 1, 2, 3, 12, 13, 14, 15)
                    : __builtin_shufflevector(w, vt[db], 0, 1, 2, 3, 12, 13, 14, 15);
    }
}

__device__ __forceinline__ float row_max(const f32x4& a, const f32x4& b) {
    float m, t;
    asm volatile("s_nop 7\n\t"
                 "v_max3_f32 %0, %2, %3, %4\n\t"
                 "v_max3_f32 %1, %6, %7, %8\n\t"
                 "v_max3_f32 %0, %0, %5, %9\n\t"
                 "v_max_f32 %0, %0, %1"
                 : "=&v"(m), "=&v"(t)
                 : "v"(a[0]), "v"(a[1]), "v"(a[2]), "v"(a[3]), "v"(b[0]), "v"(b[1]), "v"(b[2]), "v"(b[3]));
    asm volatile("v_mov_b32 %1, %0\n\ts_nop 1\n\tv_permlane16_swap_b32 %0, %1\n\tv_max_f32 %0, %0, %1\n\t"
                 "v_mov_b32 %1, %0\n\ts_nop 1\n\tv_permlane32_swap_b32 %0, %1\n\tv_max_f32 %0, %0, %1"
                 : "+v"(m), "=&v"(t));
    return m;
}
__device__ __forceinline__ float row_sum(float x) {
    const auto s = __builtin_amdgcn_permlane16_swap(__float_as_uint(x), __float_as_uint(x), false, false);
    x = __uint_as_float(s[0]) + __uint_as_float(s[1]);
    const auto t = __builtin_amdgcn_permlane32_swap(__float_as_uint(x), __float_as_uint(x), false, false);
    return __uint_as_float(t[0]) + __uint_as_float(t[1]);
}

template <bool NEW_HI>
__device__ __forceinline__ bf16x8 scores_softmax(const bf16x8 (&kold)[4], const bf16x8 (&knew)[4], const bf16x8 (&qf)[4],
                                                 f32x4 so, f32x4 sn) {
    constexpr float C2 = 0.08838834764831845f * 1.4426950408889634f;
#pragma unroll
    for (int kk = 0; kk < 4; ++kk) {
        so = __builtin_amdgcn_mfma_f32_16x16x32_bf16(kold[kk], qf[kk], so, 0, 0, 0);
        sn = __builtin_amdgcn_mfma_f32_16x16x32_bf16(knew[kk], qf[kk], sn, 0, 0, 0);
    }
    const float m = row_max(so, sn);
    float p[8];
#pragma unroll
    for (int e = 0; e < 4; ++e) {
        p[e] = __builtin_amdgcn_exp2f((so[e] - m) * C2);
        p[4 + e] = __builtin_amdgcn_exp2f((sn[e] - m) * C2);
    }
    const float inv = __builtin_amdgcn_rcpf(row_sum(((p[0] + p[1]) + (p[2] + p[3])) + ((p[4] + p[5]) + (p[6] + p[7]))));
    const unsigned po0 = pk_bf16(p[0] * inv, p[1] * inv), po1 = pk_bf16(p[2] * inv, p[3] * inv);
    const unsigned pn0 = pk_bf16(p[4] * inv, p[5] * inv), pn1 = pk_bf16(p[6] * inv, p[7] * inv);
    const u32x4 pw = NEW_HI ? u32x4{po0, po1, pn0, pn1} : u32x4{pn0, pn1, po0, po1};
    bf16x8 pf = __builtin_bit_cast(bf16x8, pw);
    asm volatile("" : "+v"(pf));
    return pf;
}

__device__ __forceinline__ void pv_tile(u32x4 (&out)[4], const bf16x8 (&vt)[8], bf16x8 pf, const unsigned (&ow)[4], unsigned orr) {
#pragma unroll
    for (int t = 0; t < 4; ++t) {
        const f32x4 a0 = __builtin_amdgcn_mfma_f32_16x16x32_bf16(vt[2 * t], pf, (f32x4){0.f, 0.f, 0.f, 0.f}, 0, 0, 0);
        const f32x4 a1 = __builtin_amdgcn_mfma_f32_16x16x32_bf16(vt[2 * t + 1], pf, (f32x4){0.f, 0.f, 0.f, 0.f}, 0, 0, 0);
        const u32x4 w = {pk_bf16(a0[0], a0[1]), pk_bf16(a0[2], a0[3]), pk_bf16(a1[0], a1[1]), pk_bf16(a1[2], a1[3])};
        *(LAS u32x4*)(size_t)(ow[t]) = w;
    }
#pragma unroll
    for (int c = 0; c < 4; ++c) out[c] = *(const LAS u32x4*)(size_t)(orr + 1024 * c);
}
__device__ __forceinline__ void store_tile(const u32x4 (&out)[4], __amdgpu_buffer_rsrc_t ro, const unsigned (&ost)[4], int off) {
#pragma unroll
    for (int c = 0; c < 4; ++c) __builtin_amdgcn_raw_buffer_store_b128(out[c], ro, ost[c] + (unsigned)off, 0, AUX_NT);
}

__global__ __launch_bounds__(256, 2) void swa_dilated(const char* __restrict__ q, const char* __restrict__ k,
                                                      const char* __restrict__ v, char* __restrict__ o) {
    extern __shared__ __attribute__((aligned(16))) char lds[];
    const u32x8 ka = *(const __attribute__((address_space(4))) u32x8*)__builtin_amdgcn_kernarg_segment_ptr();
    SBAR();
    const int lane = threadIdx.x & 63;
    const unsigned r = (unsigned)__builtin_amdgcn_readfirstlane(threadIdx.x >> 6);
    const unsigned bid = blockIdx.x;
    const unsigned wg = (bid & 7) * 32 + (bid >> 3);
    const unsigned blk = wg & 7;
    const bool asc = (blk & 1) != 0;
    const int n = lane & 15, g = lane >> 4;
    const unsigned hb = (wg >> 3) * (S * ROWB);
    const unsigned base = wg * (64 * T * ROWB) + r * ROWB;
    const int step = asc ? TILEB : -TILEB;
    const unsigned q0 = asc ? base : base + (T - 1) * TILEB;
    const unsigned kv1 = asc ? base : base + (T - 2) * TILEB;
    const unsigned wl = (unsigned)(size_t)(LAS char*)lds + r * WAVE_LDS;

    unsigned nk[4], nv[4], vqk[4], vv[4], hk[4], hv[4], fk[4], fv[4];
    const unsigned cv = (unsigned)((n ^ (g << 2)) << 4);
    const unsigned rowsel = blk == 0 ? 0u : 1u;
#pragma unroll
    for (int i = 0; i < 4; ++i) {
        const unsigned ck = (unsigned)(((n ^ g) ^ (4 * i)) << 4);
        const unsigned row = (unsigned)(g * 1024 + i * 4096);
        nk[i] = row + ck;
        nv[i] = row + cv;
        vqk[i] = nk[i] - 1024 * i;
        vv[i] = nv[i] - 1024 * i;
        hk[i] = i < 2 ? OOB : row * rowsel + ck;
        hv[i] = i < 2 ? OOB : row * rowsel + cv;
        fk[i] = asc ? hk[i] : nk[i];
        fv[i] = asc ? hv[i] : nv[i];
    }
    const unsigned halo = blk == 0 ? hb : base - TILEB;
    const unsigned first = asc ? halo : base + (T - 1) * TILEB;
    const unsigned last = asc ? base + (T - 1) * TILEB : halo;
    u32x8 kb = ka;
    asm volatile("" : "+s"(kb), "+v"(fk[0]), "+v"(fk[1]), "+v"(fk[2]), "+v"(fk[3]));
    const __amdgpu_buffer_rsrc_t rq = mk_rsrc(kb[0], kb[1]), rk = mk_rsrc(kb[2], kb[3]);
    const __amdgpu_buffer_rsrc_t rv = mk_rsrc(kb[4], kb[5]), ro = mk_rsrc(kb[6], kb[7]);

    dma_edge(wl + 0 * REGB, rk, first, fk);
    dma_edge(wl + 1 * REGB, rv, first, fv);
    dma_tile(wl + 2 * REGB, rk, kv1, vqk);
    dma_tile(wl + 3 * REGB, rq, q0, vqk);
    dma_tile(wl + 4 * REGB, rv, kv1, vv);
    dma_tile(wl + 5 * REGB, rk, kv1 + step, vqk);
    dma_tile(wl + 6 * REGB, rq, q0 + step, vqk);
    dma_tile(wl + 7 * REGB, rv, kv1 + step, vv);

    const float NEG = -__builtin_inff();
    f32x4 mold, mnew;
#pragma unroll
    for (int e = 0; e < 4; ++e) {
        const int key = 4 * g + e;
        const float mc = ((unsigned)(n - key) <= 7u) ? 0.f : NEG;
        const float mp = (key >= n + 9) ? 0.f : NEG;
        mold[e] = asc ? mp : mc;
        mnew[e] = asc ? mc : mp;
    }
    const float pre = (blk == 0) ? -1e9f * 11.313708498984761f : 0.f;
    const float pre_first = asc ? pre : 0.f, pre_last = asc ? 0.f : pre;

    unsigned fa[4], va[4], ow[4], ost[4];
    const int tq = n >> 2, tp = n & 3;
#pragma unroll
    for (int c = 0; c < 4; ++c) {
        fa[c] = wl + (unsigned)(256 * n + (((g ^ n) ^ (4 * c)) << 4));
        va[c] = wl + (unsigned)(256 * (4 * g + tq) + 64 * (c ^ tq) + 16 * tp);
        ow[c] = wl + 3 * REGB + (unsigned)(256 * n + (((4 * c + g) ^ (n & 7)) << 4));
        const int row = 4 * c + g;
        ost[c] = q0 + (unsigned)(row * 1024 + ((n ^ (row & 7)) << 4));
    }
    const unsigned orr = wl + 3 * REGB + (unsigned)(lane * 16);

    bf16x8 ka_[4], kb_[4], qf[4], vt[8], pf;
    u32x4 out0[4], out1[4], out2[4], out3[4];

    wait_dma<16>();
#pragma unroll
    for (int kk = 0; kk < 4; ++kk) {
        qf[kk] = *(const LAS bf16x8*)(size_t)(fa[kk] + 3 * REGB);
        ka_[kk] = *(const LAS bf16x8*)(size_t)(fa[kk] + 0 * REGB);
        kb_[kk] = *(const LAS bf16x8*)(size_t)(fa[kk] + 2 * REGB);
    }
    SBAR();
    wait_lds();
    dma_tile(wl + 0 * REGB, rv, kv1 + 2 * step, vv);
    pf = scores_softmax<true>(ka_, kb_, qf, mold + pre_first, mnew);
    wait_dma<16>();
#pragma unroll
    for (int db = 0; db < 8; ++db) {
        const bf16x4 vn = read_vt(va, db, 4), vo = read_vt(va, db, 1);
        vt[db] = __builtin_shufflevector(vo, vn, 0, 1, 2, 3, 4, 5, 6, 7);
    }
    SBAR();
    wait_lds();
    dma_tile(wl + 1 * REGB, rk, kv1 + 2 * step, vqk);
    dma_tile(wl + 2 * REGB, rq, q0 + 2 * step, vqk);
    pv_tile(out0, vt, pf, ow, orr);

    wait_dma<16>();
#pragma unroll
    for (int kk = 0; kk < 4; ++kk) {
        qf[kk] = *(const LAS bf16x8*)(size_t)(fa[kk] + 6 * REGB);
        ka_[kk] = *(const LAS bf16x8*)(size_t)(fa[kk] + 5 * REGB);
    }
    SBAR();
    wait_lds();
    {
        unsigned x[4];
#pragma unroll
        for (int i = 0; i < 4; ++i) x[i] = asc ? nv[i] : hv[i];
        dma_edge(wl + 4 * REGB, rv, last, x);
    }
    pf = scores_softmax<false>(kb_, ka_, qf, mold, mnew);
    wait_dma<16>();
    read_v<false>(vt, va, 7);
    SBAR();
    wait_lds();
    {
        unsigned x[4];
#pragma unroll
        for (int i = 0; i < 4; ++i) x[i] = asc ? nk[i] : hk[i];
        dma_edge(wl + 5 * REGB, rk, last, x);
    }
    dma_tile(wl + 6 * REGB, rq, q0 + 3 * step, vqk);
    pv_tile(out1, vt, pf, ow, orr);

    wait_dma<12>();
#pragma unroll
    for (int kk = 0; kk < 4; ++kk) {
        qf[kk] = *(const LAS bf16x8*)(size_t)(fa[kk] + 2 * REGB);
        kb_[kk] = *(const LAS bf16x8*)(size_t)(fa[kk] + 1 * REGB);
    }
    SBAR();
    pf = scores_softmax<true>(ka_, kb_, qf, mold, mnew);
    wait_dma<20>();
    read_v<true>(vt, va, 0);
    SBAR();
    pv_tile(out2, vt, pf, ow, orr);

    wait_dma<0>();
#pragma unroll
    for (int kk = 0; kk < 4; ++kk) {
        qf[kk] = *(const LAS bf16x8*)(size_t)(fa[kk] + 6 * REGB);
        ka_[kk] = *(const LAS bf16x8*)(size_t)(fa[kk] + 5 * REGB);
    }
    SBAR();
    pf = scores_softmax<false>(kb_, ka_, qf, mold, mnew + pre_last);
    read_v<false>(vt, va, 4);
    SBAR();
    pv_tile(out3, vt, pf, ow, orr);

    store_tile(out0, ro, ost, 0);
    store_tile(out1, ro, ost, step);
    store_tile(out2, ro, ost, 2 * step);
    store_tile(out3, ro, ost, 3 * step);
}

extern "C" void kernel_launch(void* const* d_in, const int* in_sizes, int n_in, void* d_out, int out_size,
                              void* d_ws, size_t ws_size, hipStream_t stream) {
    (void)hipFuncSetAttribute((const void*)swa_dilated, hipFuncAttributeMaxDynamicSharedMemorySize, LDS_BYTES);
    swa_dilated<<<H * S / (64 * T), 256, LDS_BYTES, stream>>>((const char*)d_in[0], (const char*)d_in[1],
                                                              (const char*)d_in[2], (char*)d_out);
}
```
